```python
import torch

def _softmax(x, axis=-1):
    return torch.softmax(x, dim=axis)
def _gelu(x):
    return torch.nn.functional.gelu(x, approximate="tanh")
def _silu(x):
    return torch.nn.functional.silu(x)
def _sigmoid(x):
    return torch.sigmoid(x)
def _layernorm(x, axis=-1, eps=1e-5):
    m = x.mean(dim=axis, keepdim=True)
    v = x.var(dim=axis, keepdim=True, unbiased=False)
    return (x - m) / torch.sqrt(v + eps)
def _scatter_add(tgt, idx, src, axis=0):
    return tgt.clone().index_add_(axis, idx, src)
def _segment_sum(src, seg, n):
    out = torch.zeros((n,) + src.shape[1:], dtype=src.dtype)
    return out.index_add_(0, seg, src)
def _pad(x, pw):
    flat = tuple(v for pair in reversed(pw) for v in pair)
    return torch.nn.functional.pad(x, flat)

kernel_name = 'standalone__weight_only_int_gptq__prompt'

def setup_inputs():

    _w = (lambda a: (lambda t: t.to(torch.bfloat16) if t.is_floating_point() else t)(torch.as_tensor(a)))
    B, S, D, F = 1, 2048, 576, 4608
    bits = max(2, min(4, 3))
    pf = 32 // bits
    G = -1 if -1 > 0 else D
    G = max(pf, (G // pf) * pf)
    D = max(G, (D // G) * G)
    ng = D // G
    qmax = 1 << bits
    qw = torch.randint(0, qmax, (D, F)).to(torch.int64)
    packed = torch.zeros((D // pf, F), dtype=torch.int64)
    for i in range(pf):
        packed |= (qw[i::pf] << (i * bits))
    packed = packed.to(torch.int32)
    return {
        "x": _w((lambda *s: torch.randn(*s, dtype=torch.bfloat16) * 0.05)(B, S, D)),
        "qw_packed": _w(packed),
        "scales": _w(torch.abs((lambda *s: torch.randn(*s, dtype=torch.bfloat16) * 0.05)(ng, F)).to(float) * 0.1 + 0.01),
        "zeros": _w(torch.randint(0, qmax, (ng, F)).to(float)),
        "bits": _w((lambda _x: torch.as_tensor(_x, dtype=torch.int32))(bits)),
    }

def reference(x, qw_packed, scales, zeros, bits):
    b = int(bits)
    pf = 32 // b
    mask = (1 << b) - 1
    P, F = qw_packed.shape
    D = P * pf
    ng = scales.shape[0]
    G = D // ng
    shifts = (lambda x, dt: x.to(dt))(torch.arange(pf) * b, qw_packed.dtype)
    qw = ((qw_packed[:, None, :] >> shifts[None, :, None]) & mask).reshape(D, F)
    s = (lambda x, n, axis: torch.repeat_interleave(x, n, dim=axis))(scales, G, axis=0)
    z = (lambda x, n, axis: torch.repeat_interleave(x, n, dim=axis))(zeros, G, axis=0)
    w = ((lambda x, dt: x.to(dt))(qw, x.dtype) - z) * s
    return torch.einsum("bsd,df->bsf", x, w)
```

```cpp
#include <hip/hip_runtime.h>
#include <hip/hip_bf16.h>


typedef __attribute__((ext_vector_type(8))) short bf16x8;
typedef __attribute__((ext_vector_type(4))) float f32x4;
typedef __attribute__((ext_vector_type(4))) unsigned int u32x4;
typedef __attribute__((ext_vector_type(2))) unsigned int u32x2;

constexpr int S_ = 2048, D_ = 570, F_ = 4608, P_ = 57;
constexpr int BM = 256, BN = 144;
constexpr int NTHREADS = 512;
constexpr int ROWB = 336;
constexpr int BUFB = BN * ROWB;
constexpr int LDS_BYTES = 2 * BUFB;

__device__ __forceinline__ unsigned int pack_bf16(float lo, float hi) {
    __bf16 a = (__bf16)lo, b = (__bf16)hi;
    unsigned short ua = __builtin_bit_cast(unsigned short, a), ub = __builtin_bit_cast(unsigned short, b);
    return (unsigned int)ua | ((unsigned int)ub << 16);
}
__device__ __forceinline__ float bf16_round(float f) { return (float)(__bf16)f; }

__device__ __forceinline__ unsigned int spread4(unsigned int x) {
    unsigned int y = (x & 0x3Fu) | ((x << 10) & 0x3F0000u);
    return (y & 0x00070007u) | ((y << 5) & 0x07000700u);
}

__global__ __launch_bounds__(NTHREADS, 2)
void gptq_gemm_kernel(const unsigned short* __restrict__ x, const int* __restrict__ qw,
                      const unsigned short* __restrict__ scales, const unsigned short* __restrict__ zeros,
                      unsigned short* __restrict__ out) {
    extern __shared__ __attribute__((aligned(16))) char lds[];
    const int tid = threadIdx.x;
    const int wid = tid >> 6, lane = tid & 63;
    const int bid = blockIdx.x;
    const int tn = bid & 31, tm = bid >> 5;
    const int n0 = tn * BN, m0 = tm * BM;

    const bool dq = tid < 3 * BN;
    const int dg = dq ? tid / BN : 0;
    const int dcol = dq ? tid - dg * BN : 0;
    unsigned int L0 = 0, L1 = 0, H0 = 0, H1 = 0;
    {
        float s = __uint_as_float((unsigned int)scales[n0 + dcol] << 16);
        float z = __uint_as_float((unsigned int)zeros[n0 + dcol] << 16);
        unsigned int wq[8];
#pragma unroll
        for (int q = 0; q < 8; ++q) wq[q] = pack_bf16(bf16_round((float)q - z) * s, 0.f) & 0xFFFFu;
#pragma unroll
        for (int q = 0; q < 4; ++q) {
            L0 |= (wq[q] & 0xFFu) << (8 * q);       H0 |= (wq[q] >> 8) << (8 * q);
            L1 |= (wq[q + 4] & 0xFFu) << (8 * q);   H1 |= (wq[q + 4] >> 8) << (8 * q);
        }
    }
    const int* qcol = qw + n0 + dcol;

    auto dequant_word = [&](unsigned int p, char* dst) {
        unsigned int sa = spread4(p), sb = spread4(p >> 12);
        unsigned int sc = ((p >> 24) & 7u) | (((p >> 27) & 7u) << 8);
        unsigned int la = __builtin_amdgcn_perm(L1, L0, sa), ha = __builtin_amdgcn_perm(H1, H0, sa);
        unsigned int lb = __builtin_amdgcn_perm(L1, L0, sb), hb = __builtin_amdgcn_perm(H1, H0, sb);
        unsigned int lc = __builtin_amdgcn_perm(L1, L0, sc), hc = __builtin_amdgcn_perm(H1, H0, sc);
        unsigned int* d = (unsigned int*)dst;
        d[0] = __builtin_amdgcn_perm(ha, la, 0x05010400u);
        d[1] = __builtin_amdgcn_perm(ha, la, 0x07030602u);
        d[2] = __builtin_amdgcn_perm(hb, lb, 0x05010400u);
        d[3] = __builtin_amdgcn_perm(hb, lb, 0x07030602u);
        d[4] = __builtin_amdgcn_perm(hc, lc, 0x05010400u);
    };
    auto dequant_chunk = [&](int c, char* buf) {
        if (dq) {
            const int first = (dg + 3 * 16 - (16 * c) % 3) % 3;
            const int nw = (c < 3) ? 16 : 9;
            unsigned int p[6];
#pragma unroll
            for (int i = 0; i < 6; ++i) {
                int w = first + 3 * i;
                p[i] = (w < nw) ? (unsigned int)qcol[(16 * c + w) * F_] : 0u;
            }
#pragma unroll
            for (int i = 0; i < 6; ++i) {
                int w = first + 3 * i;
                if (w < nw) dequant_word(p[i], buf + dcol * ROWB + w * 20);
            }
            if (c == 3 && dg == 0) {
                unsigned int* dst = (unsigned int*)(buf + dcol * ROWB + 180);
                dst[0] = 0; dst[1] = 0; dst[2] = 0;
            }
        }
    };

    dequant_chunk(0, lds);

    const int fr = lane & 15, fq = lane >> 4;
    const unsigned short* xrow0 = x + (size_t)(m0 + wid * 32 + fr) * D_ + fq * 8;
    const unsigned short* xrow1 = xrow0 + 16 * D_;

    auto loadA = [&](const unsigned short* row, int ks) -> bf16x8 {
        typedef unsigned int u32x4a __attribute__((ext_vector_type(4), aligned(4)));
        u32x4 v;
        if (ks < 17) {
            v = *(const u32x4a*)(row + ks * 32);
        } else {
            v = (u32x4){0u, 0u, 0u, 0u};
            if (fq < 3) v = *(const u32x4a*)(row + ks * 32);
            else v[0] = *(const unsigned int*)(row + ks * 32);
        }
        return __builtin_bit_cast(bf16x8, v);
    };

    f32x4 acc[2][9];
#pragma unroll
    for (int i = 0; i < 2; ++i)
#pragma unroll
        for (int j = 0; j < 9; ++j) acc[i][j] = (f32x4){0.f, 0.f, 0.f, 0.f};

    bf16x8 a0[3], a1[3];
    a0[0] = loadA(xrow0, 0); a1[0] = loadA(xrow1, 0);
    a0[1] = loadA(xrow0, 1); a1[1] = loadA(xrow1, 1);

    __syncthreads();

#pragma unroll
    for (int c = 0; c < 4; ++c) {
        const char* buf = lds + (c & 1) * BUFB + fr * ROWB + fq * 16;
        const int nks = (c < 3) ? 5 : 3;
#pragma unroll
        for (int s = 0; s < nks; ++s) {
            const int ks = c * 5 + s;
            if (ks + 2 < 18) { a0[(ks + 2) % 3] = loadA(xrow0, ks + 2); a1[(ks + 2) % 3] = loadA(xrow1, ks + 2); }
#pragma unroll
            for (int j = 0; j < 9; ++j) {
                bf16x8 b = *(const bf16x8*)(buf + j * 16 * ROWB + s * 64);
                acc[0][j] = __builtin_amdgcn_mfma_f32_16x16x32_bf16(b, a0[ks % 3], acc[0][j], 0, 0, 0);
                acc[1][j] = __builtin_amdgcn_mfma_f32_16x16x32_bf16(b, a1[ks % 3], acc[1][j], 0, 0, 0);
            }
        }
        if (c < 3) { dequant_chunk(c + 1, lds + ((c + 1) & 1) * BUFB); __syncthreads(); }
    }

#pragma unroll
    for (int i = 0; i < 2; ++i) {
        unsigned short* orow = out + (size_t)(m0 + wid * 32 + i * 16 + fr) * F_ + n0 + fq * 4;
#pragma unroll
        for (int j = 0; j < 9; ++j) {
            u32x2 v;
            v[0] = pack_bf16(acc[i][j][0], acc[i][j][1]);
            v[1] = pack_bf16(acc[i][j][2], acc[i][j][3]);
            *(u32x2*)(orow + j * 16) = v;
        }
    }
}

extern "C" void kernel_launch(void* const* d_in, const int* in_sizes, int n_in,
                              void* d_out, int out_size, void* d_ws, size_t ws_size,
                              hipStream_t stream) {
    const unsigned short* x = (const unsigned short*)d_in[0];
    const int* qw = (const int*)d_in[1];
    const unsigned short* scales = (const unsigned short*)d_in[2];
    const unsigned short* zeros = (const unsigned short*)d_in[3];
    unsigned short* out = (unsigned short*)d_out;
    (void)hipFuncSetAttribute((const void*)gptq_gemm_kernel, hipFuncAttributeMaxDynamicSharedMemorySize, LDS_BYTES);
    gptq_gemm_kernel<<<256, NTHREADS, LDS_BYTES, stream>>>(x, qw, scales, zeros, out);
}
```

```cpp
#include <hip/hip_runtime.h>
#include <hip/hip_bf16.h>


typedef __attribute__((ext_vector_type(8))) short bf16x8;
typedef __attribute__((ext_vector_type(4))) float f32x4;
typedef __attribute__((ext_vector_type(4))) unsigned int u32x4;
typedef __attribute__((ext_vector_type(2))) unsigned int u32x2;

constexpr int S_ = 2048, D_ = 570, F_ = 4608, P_ = 57;
constexpr int BM = 256, BN = 144;
constexpr int NTHREADS = 512;
constexpr int ROWB = 336;
constexpr int BUFB = BN * ROWB;
constexpr int LDS_BYTES = 2 * BUFB;

__device__ __forceinline__ unsigned int pack_bf16(float lo, float hi) {
    __bf16 a = (__bf16)lo, b = (__bf16)hi;
    unsigned short ua = __builtin_bit_cast(unsigned short, a), ub = __builtin_bit_cast(unsigned short, b);
    return (unsigned int)ua | ((unsigned int)ub << 16);
}
__device__ __forceinline__ float bf16_round(float f) { return (float)(__bf16)f; }

__device__ __forceinline__ unsigned int spread4(unsigned int x) {
    unsigned int y = (x & 0x3Fu) | ((x << 10) & 0x3F0000u);
    return (y & 0x00070007u) | ((y << 5) & 0x07000700u);
}

__global__ __launch_bounds__(NTHREADS, 2)
void gptq_gemm_kernel(const unsigned short* __restrict__ x, const int* __restrict__ qw,
                      const unsigned short* __restrict__ scales, const unsigned short* __restrict__ zeros,
                      unsigned short* __restrict__ out) {
    extern __shared__ __attribute__((aligned(16))) char lds[];
    const int tid = threadIdx.x;
    const int wid = tid >> 6, lane = tid & 63;
    const int bid = blockIdx.x;
    const int tn = bid & 31, tm = bid >> 5;
    const int n0 = tn * BN, m0 = tm * BM;

    const bool dq = tid < 3 * BN;
    const int dg = dq ? tid / BN : 0;
    const int dcol = dq ? tid - dg * BN : 0;
    unsigned int L0 = 0, L1 = 0, H0 = 0, H1 = 0;
    {
        float s = __uint_as_float((unsigned int)scales[n0 + dcol] << 16);
        float z = __uint_as_float((unsigned int)zeros[n0 + dcol] << 16);
        unsigned int wq[8];
#pragma unroll
        for (int q = 0; q < 8; ++q) wq[q] = pack_bf16(bf16_round((float)q - z) * s, 0.f) & 0xFFFFu;
#pragma unroll
        for (int q = 0; q < 4; ++q) {
            L0 |= (wq[q] & 0xFFu) << (8 * q);       H0 |= (wq[q] >> 8) << (8 * q);
            L1 |= (wq[q + 4] & 0xFFu) << (8 * q);   H1 |= (wq[q + 4] >> 8) << (8 * q);
        }
    }
    const int* qcol = qw + n0 + dcol;

    auto dequant_word = [&](unsigned int p, char* dst) {
        unsigned int sa = spread4(p), sb = spread4(p >> 12);
        unsigned int sc = ((p >> 24) & 7u) | (((p >> 27) & 7u) << 8);
        unsigned int la = __builtin_amdgcn_perm(L1, L0, sa), ha = __builtin_amdgcn_perm(H1, H0, sa);
        unsigned int lb = __builtin_amdgcn_perm(L1, L0, sb), hb = __builtin_amdgcn_perm(H1, H0, sb);
        unsigned int lc = __builtin_amdgcn_perm(L1, L0, sc), hc = __builtin_amdgcn_perm(H1, H0, sc);
        unsigned int* d = (unsigned int*)dst;
        d[0] = __builtin_amdgcn_perm(ha, la, 0x05010400u);
        d[1] = __builtin_amdgcn_perm(ha, la, 0x07030602u);
        d[2] = __builtin_amdgcn_perm(hb, lb, 0x05010400u);
        d[3] = __builtin_amdgcn_perm(hb, lb, 0x07030602u);
        d[4] = __builtin_amdgcn_perm(hc, lc, 0x05010400u);
    };
    unsigned int p[6];
    auto load_chunk = [&](int c) {
        const int first = (dg + 3 * 16 - (16 * c) % 3) % 3;
        const int nw = (c < 3) ? 16 : 9;
#pragma unroll
        for (int i = 0; i < 6; ++i) {
            int w = first + 3 * i;
            p[i] = (dq && w < nw) ? (unsigned int)qcol[(16 * c + w) * F_] : 0u;
        }
    };
    auto dequant_chunk = [&](int c, char* buf) {
        if (dq) {
            const int first = (dg + 3 * 16 - (16 * c) % 3) % 3;
            const int nw = (c < 3) ? 16 : 9;
#pragma unroll
            for (int i = 0; i < 6; ++i) {
                int w = first + 3 * i;
                if (w < nw) dequant_word(p[i], buf + dcol * ROWB + w * 20);
            }
            if (c == 3 && dg == 0) {
                unsigned int* dst = (unsigned int*)(buf + dcol * ROWB + 180);
                dst[0] = 0; dst[1] = 0; dst[2] = 0;
            }
        }
    };

    load_chunk(0);
    dequant_chunk(0, lds);

    const int fr = lane & 15, fq = lane >> 4;
    const unsigned short* xrow0 = x + (size_t)(m0 + wid * 32 + fr) * D_ + fq * 8;
    const unsigned short* xrow1 = xrow0 + 16 * D_;

    auto loadA = [&](const unsigned short* row, int ks) -> bf16x8 {
        typedef unsigned int u32x4a __attribute__((ext_vector_type(4), aligned(4)));
        u32x4 v;
        if (ks < 17) {
            v = *(const u32x4a*)(row + ks * 32);
        } else {
            v = (u32x4){0u, 0u, 0u, 0u};
            if (fq < 3) v = *(const u32x4a*)(row + ks * 32);
            else v[0] = *(const unsigned int*)(row + ks * 32);
        }
        return __builtin_bit_cast(bf16x8, v);
    };

    f32x4 acc[2][9];
#pragma unroll
    for (int i = 0; i < 2; ++i)
#pragma unroll
        for (int j = 0; j < 9; ++j) acc[i][j] = (f32x4){0.f, 0.f, 0.f, 0.f};

    bf16x8 a0[3], a1[3];
    a0[0] = loadA(xrow0, 0); a1[0] = loadA(xrow1, 0);
    a0[1] = loadA(xrow0, 1); a1[1] = loadA(xrow1, 1);

    __syncthreads();

#pragma unroll
    for (int c = 0; c < 4; ++c) {
        const char* buf = lds + (c & 1) * BUFB + fr * ROWB + fq * 16;
        const int nks = (c < 3) ? 5 : 3;
        if (c < 3) load_chunk(c + 1);
#pragma unroll
        for (int s = 0; s < nks; ++s) {
            const int ks = c * 5 + s;
            if (ks + 2 < 18) { a0[(ks + 2) % 3] = loadA(xrow0, ks + 2); a1[(ks + 2) % 3] = loadA(xrow1, ks + 2); }
#pragma unroll
            for (int j = 0; j < 9; ++j) {
                bf16x8 b = *(const bf16x8*)(buf + j * 16 * ROWB + s * 64);
                acc[0][j] = __builtin_amdgcn_mfma_f32_16x16x32_bf16(b, a0[ks % 3], acc[0][j], 0, 0, 0);
                acc[1][j] = __builtin_amdgcn_mfma_f32_16x16x32_bf16(b, a1[ks % 3], acc[1][j], 0, 0, 0);
            }
        }
        if (c < 3) { dequant_chunk(c + 1, lds + ((c + 1) & 1) * BUFB); __syncthreads(); }
    }

    __syncthreads();
    char* ob = lds + wid * (32 * 304);
#pragma unroll
    for (int i = 0; i < 2; ++i)
#pragma unroll
        for (int j = 0; j < 9; ++j) {
            u32x2 v;
            v[0] = pack_bf16(acc[i][j][0], acc[i][j][1]);
            v[1] = pack_bf16(acc[i][j][2], acc[i][j][3]);
            *(u32x2*)(ob + (i * 16 + fr) * 304 + (j * 16 + fq * 4) * 2) = v;
        }
    __builtin_amdgcn_s_waitcnt(0xC07F);
#pragma unroll
    for (int it = 0; it < 9; ++it) {
        int e = it * 64 + lane;
        int r = e / 18, pc = e - r * 18;
        u32x4 v = *(const u32x4*)(ob + r * 304 + pc * 16);
        *(u32x4*)(out + (size_t)(m0 + wid * 32 + r) * F_ + n0 + pc * 8) = v;
    }
}

extern "C" void kernel_launch(void* const* d_in, const int* in_sizes, int n_in,
                              void* d_out, int out_size, void* d_ws, size_t ws_size,
                              hipStream_t stream) {
    const unsigned short* x = (const unsigned short*)d_in[0];
    const int* qw = (const int*)d_in[1];
    const unsigned short* scales = (const unsigned short*)d_in[2];
    const unsigned short* zeros = (const unsigned short*)d_in[3];
    unsigned short* out = (unsigned short*)d_out;
    (void)hipFuncSetAttribute((const void*)gptq_gemm_kernel, hipFuncAttributeMaxDynamicSharedMemorySize, LDS_BYTES);
    gptq_gemm_kernel<<<256, NTHREADS, LDS_BYTES, stream>>>(x, qw, scales, zeros, out);
}
```
